```python
import torch

def _softmax(x, axis=-1):
    return torch.softmax(x, dim=axis)
def _gelu(x):
    return torch.nn.functional.gelu(x, approximate="tanh")
def _silu(x):
    return torch.nn.functional.silu(x)
def _sigmoid(x):
    return torch.sigmoid(x)
def _layernorm(x, axis=-1, eps=1e-5):
    m = x.mean(dim=axis, keepdim=True)
    v = x.var(dim=axis, keepdim=True, unbiased=False)
    return (x - m) / torch.sqrt(v + eps)
def _scatter_add(tgt, idx, src, axis=0):
    return tgt.clone().index_add_(axis, idx, src)
def _segment_sum(src, seg, n):
    out = torch.zeros((n,) + src.shape[1:], dtype=src.dtype)
    return out.index_add_(0, seg, src)
def _pad(x, pw):
    flat = tuple(v for pair in reversed(pw) for v in pair)
    return torch.nn.functional.pad(x, flat)

kernel_name = 'gated_mix__spectral_fnet__glu_gated_glu__prompt'

def setup_inputs():

    _w = (lambda a: (lambda t: t.to(torch.bfloat16) if t.is_floating_point() else t)(torch.as_tensor(a)))
    def _A_setup():
        B, S, D = 1, 2048, 512
        ns = torch.arange(S)[:, None] * torch.arange(S)[None, :]
        nd = torch.arange(D)[:, None] * torch.arange(D)[None, :]
        Ws_re = torch.cos(-2.0 * torch.pi * ns / S).to(float)
        Ws_im = torch.sin(-2.0 * torch.pi * ns / S).to(float)
        Wd_re = torch.cos(-2.0 * torch.pi * nd / D).to(float)
        Wd_im = torch.sin(-2.0 * torch.pi * nd / D).to(float)
        return {
            "x": _w((lambda *s: torch.randn(*s, dtype=torch.bfloat16) * 0.05)(B, S, D)),
            "dft_seq_re": _w(Ws_re),
            "dft_seq_im": _w(Ws_im),
            "dft_hid_re": _w(Wd_re),
            "dft_hid_im": _w(Wd_im),
        }
    _A_in = _A_setup()

    def _B_setup():
        B, S, D, F = 1, 2048, 512, 1792
        return {
            "x": _w((lambda *s: torch.randn(*s, dtype=torch.bfloat16) * 0.05)(B, S, D)),
            "w_gate": _w((lambda *s: torch.randn(*s, dtype=torch.bfloat16) * 1.6)(D, F)),
            "w_up": _w((lambda *s: torch.randn(*s, dtype=torch.bfloat16) * 0.8)(D, F)),
            "w_down": _w((lambda *s: torch.randn(*s, dtype=torch.bfloat16) * 0.8)(F, D)),
        }
    _B_in = _B_setup()

    return {
        "A_x": _A_in["x"],
        "A_dft_seq_re": _A_in["dft_seq_re"],
        "A_dft_seq_im": _A_in["dft_seq_im"],
        "A_dft_hid_re": _A_in["dft_hid_re"],
        "A_dft_hid_im": _A_in["dft_hid_im"],
        "B_x": _B_in["x"],
        "B_w_gate": _B_in["w_gate"],
        "B_w_up": _B_in["w_up"],
        "B_w_down": _B_in["w_down"],
        "gate_w": _w((lambda *s: torch.randn(*s, dtype=torch.bfloat16) * 0.05)(512) * 16.0),
    }

def reference(A_x, A_dft_seq_re, A_dft_seq_im, A_dft_hid_re, A_dft_hid_im, B_x, B_w_gate, B_w_up, B_w_down, gate_w):
    def _A_ref(x, dft_seq_re, dft_seq_im, dft_hid_re, dft_hid_im):
        a = torch.einsum("bsd,ed->bse", x, dft_hid_re)
        b = torch.einsum("bsd,ed->bse", x, dft_hid_im)
        y = torch.einsum("ts,bse->bte", dft_seq_re, a) - torch.einsum("ts,bse->bte", dft_seq_im, b)
        return y

    def _B_ref(x, w_gate, w_up, w_down):
        g = torch.einsum("bsd,df->bsf", x, w_gate)
        u = torch.einsum("bsd,df->bsf", x, w_up)
        h = _sigmoid(g) * u
        return torch.einsum("bsf,fd->bsd", h, w_down)

    x = A_x
    g = _sigmoid((x * gate_w).sum(-1, keepdims=True))
    a = _A_ref(x, A_dft_seq_re, A_dft_seq_im, A_dft_hid_re, A_dft_hid_im)
    b = _B_ref(x, B_w_gate, B_w_up, B_w_down)
    return g * a + (1 - g) * b
```

```cpp
#include <hip/hip_runtime.h>
#include <hip/hip_bf16.h>

#define LAS __attribute__((address_space(3)))
typedef unsigned short bf16_t;
typedef short bf16x8 __attribute__((ext_vector_type(8)));
typedef short bf16x4 __attribute__((ext_vector_type(4)));
typedef float f32x4 __attribute__((ext_vector_type(4)));
typedef unsigned u32x4 __attribute__((ext_vector_type(4)));
typedef unsigned u32x2 __attribute__((ext_vector_type(2)));
typedef LAS char* lds_ptr;

constexpr int S_ = 2048, D_ = 512, F_ = 1792, ZLD = 2816;

#define WAIT_VM(n) asm volatile("s_waitcnt vmcnt(%0)" ::"i"(n) : "memory")
#define WAIT_LGKM0() asm volatile("s_waitcnt lgkmcnt(0)" ::: "memory")
#define BAR() do { asm volatile("" ::: "memory"); __builtin_amdgcn_s_barrier(); asm volatile("" ::: "memory"); } while (0)

__device__ __forceinline__ void dma16(const char* gsrc, unsigned ldsaddr) {
    asm volatile("s_mov_b32 m0, %1\n\ts_nop 0\n\tglobal_load_lds_dwordx4 %0, off" ::"v"(gsrc), "s"(ldsaddr) : "memory");
}
__device__ __forceinline__ bf16x8 lds_row_frag(lds_ptr p) { return *(LAS bf16x8*)p; }
__device__ __forceinline__ bf16x8 lds_tr_frag(lds_ptr p, int off2) {
    bf16x4 a = __builtin_amdgcn_ds_read_tr16_b64_v4i16((LAS bf16x4*)p);
    bf16x4 b = __builtin_amdgcn_ds_read_tr16_b64_v4i16((LAS bf16x4*)(p + off2));
    return __builtin_shufflevector(a, b, 0, 1, 2, 3, 4, 5, 6, 7);
}
__device__ __forceinline__ f32x4 mfma16(bf16x8 a, bf16x8 b, f32x4 c) { return __builtin_amdgcn_mfma_f32_16x16x32_bf16(a, b, c, 0, 0, 0); }
__device__ __forceinline__ unsigned pk_bf16(float lo, float hi) {
    unsigned r; asm("v_cvt_pk_bf16_f32 %0, %1, %2" : "=v"(r) : "v"(lo), "v"(hi)); return r;
}
__device__ __forceinline__ float sigmoidf_(float v) { return __builtin_amdgcn_rcpf(1.0f + __builtin_amdgcn_exp2f(-1.44269504088896f * v)); }

__device__ __forceinline__ int swz_k256(int k) { return (((k & 3) | (((k >> 3) & 1) << 2)) << 1); }
__device__ __forceinline__ int swz_k128(int k) { return ((((k >> 1) & 1) | (((k >> 3) & 1) << 1)) << 1); }

struct Params {
    const bf16_t* x; const bf16_t* cr; const bf16_t* ci; const bf16_t* hr; const bf16_t* hi;
    const bf16_t* wg; const bf16_t* wu; const bf16_t* wd; const bf16_t* gw;
    bf16_t* z; float* g; bf16_t* out;
};

constexpr int A1_ST = 4, A1_STAGE = 32768, A1_NT = 32;
__device__ __forceinline__ void a1_tile(const Params& p, lds_ptr smem, int mat, int row0, int col0, int wid, int lane) {
    const unsigned sbase = (unsigned)(size_t)smem;
    if (wid >= 4) {
        const int lw = wid - 4;
        const bf16_t* C = mat ? p.ci : p.cr;
        const char* srcA[4]; const char* srcB[4];
#pragma unroll
        for (int i = 0; i < 4; ++i) {
            const int piece = lw * 4 + i;
            const int r = piece * 8 + (lane >> 3), c = (lane & 7) ^ ((r >> 1) & 7);
            srcA[i] = (const char*)(C + (size_t)(row0 + r) * S_) + c * 16;
            const int k = piece * 4 + (lane >> 4), cc = (lane & 15) ^ swz_k256(k);
            srcB[i] = (const char*)(p.x + (size_t)k * D_ + col0) + cc * 16;
        }
        auto issue = [&](int slot) {
#pragma unroll
            for (int i = 0; i < 4; ++i) { dma16(srcA[i], sbase + slot * A1_STAGE + (lw * 4 + i) * 1024); srcA[i] += 128; }
#pragma unroll
            for (int i = 0; i < 4; ++i) { dma16(srcB[i], sbase + slot * A1_STAGE + 16384 + (lw * 4 + i) * 1024); srcB[i] += 64 * D_ * 2; }
        };
#pragma unroll
        for (int s = 0; s < A1_ST; ++s) issue(s);
        WAIT_VM((A1_ST - 1) * 8); BAR();
        for (int t = 0; t < A1_NT - A1_ST; ++t) {
            WAIT_VM((A1_ST - 2) * 8); BAR();
            issue(t % A1_ST);
        }
        WAIT_VM(16); BAR();
        WAIT_VM(8); BAR();
        WAIT_VM(0); BAR();
        BAR();
        return;
    }
    const int wm = wid >> 1, wn = wid & 1;
    const int fr = lane & 15, q = lane >> 4, qq = fr >> 2, pp = lane & 3;
    int offM[2];
#pragma unroll
    for (int kk = 0; kk < 2; ++kk) offM[kk] = (wm * 64 + fr) * 128 + (((kk * 4 + q) ^ (fr >> 1)) * 16);
    int offN[4];
    const int f = qq | ((q & 1) << 2);
#pragma unroll
    for (int ni = 0; ni < 4; ++ni) offN[ni] = 16384 + (8 * q + qq) * 256 + 32 * ((wn * 4 + ni) ^ f) + 8 * pp;
    f32x4 acc[4][4];
#pragma unroll
    for (int a = 0; a < 4; ++a)
#pragma unroll
        for (int b = 0; b < 4; ++b) acc[a][b] = (f32x4){0.f, 0.f, 0.f, 0.f};
    bf16x8 M0[4], N0[4], M1[4], N1[4];
    auto rd = [&](bf16x8(&M)[4], bf16x8(&N)[4], int slot, int kk) {
        lds_ptr s = smem + slot * A1_STAGE;
#pragma unroll
        for (int i = 0; i < 4; ++i) N[i] = lds_tr_frag(s + offN[i] + kk * 8192, 1024);
#pragma unroll
        for (int i = 0; i < 4; ++i) M[i] = lds_row_frag(s + offM[kk] + i * 2048);
    };
    auto mm = [&](bf16x8(&M)[4], bf16x8(&N)[4]) {
#pragma unroll
        for (int mi = 0; mi < 4; ++mi)
#pragma unroll
            for (int ni = 0; ni < 4; ++ni) acc[ni][mi] = mfma16(N[ni], M[mi], acc[ni][mi]);
    };
    BAR();
    rd(M0, N0, 0, 0);
#pragma unroll 1
    for (int t4 = 0; t4 < A1_NT; t4 += A1_ST) {
#pragma unroll
        for (int s = 0; s < A1_ST; ++s) {
            rd(M1, N1, s, 1);
            mm(M0, N0);
            WAIT_LGKM0(); BAR();
            if (s + 1 < A1_ST || t4 + A1_ST < A1_NT) rd(M0, N0, (s + 1) % A1_ST, 0);
            mm(M1, N1);
        }
    }
    const float sg = mat ? -1.f : 1.f;
    bf16_t* zb = p.z + (size_t)(row0 + wm * 64 + fr) * ZLD + mat * 512 + col0 + wn * 64 + 4 * q;
#pragma unroll
    for (int mi = 0; mi < 4; ++mi)
#pragma unroll
        for (int ni = 0; ni < 4; ++ni) {
            const f32x4 v = acc[ni][mi] * sg;
            u32x2 w; w.x = pk_bf16(v[0], v[1]); w.y = pk_bf16(v[2], v[3]);
            *(u32x2*)(zb + (size_t)mi * 16 * ZLD + ni * 16) = w;
        }
}

constexpr int B1_STAGE = 65536, B1_NT = 8;
__device__ __forceinline__ void b1_tile(const Params& p, lds_ptr smem, int row0, int f0, int wid, int lane) {
    const unsigned sbase = (unsigned)(size_t)smem;
    const char* srcA[4]; const char* srcG[2]; const char* srcU[2];
#pragma unroll
    for (int i = 0; i < 4; ++i) {
        const int piece = wid * 4 + i;
        const int r = piece * 8 + (lane >> 3), c = (lane & 7) ^ ((r >> 1) & 7);
        srcA[i] = (const char*)(p.x + (size_t)(row0 + r) * D_) + c * 16;
    }
#pragma unroll
    for (int i = 0; i < 2; ++i) {
        const int piece = wid * 2 + i;
        const int k = piece * 4 + (lane >> 4), cc = (lane & 15) ^ swz_k256(k);
        srcG[i] = (const char*)(p.wg + (size_t)k * F_ + f0) + cc * 16;
        srcU[i] = (const char*)(p.wu + (size_t)k * F_ + f0) + cc * 16;
    }
    auto issue = [&](int slot) {
#pragma unroll
        for (int i = 0; i < 2; ++i) { dma16(srcG[i], sbase + slot * B1_STAGE + 32768 + (wid * 2 + i) * 1024); srcG[i] += 64 * F_ * 2; }
#pragma unroll
        for (int i = 0; i < 2; ++i) { dma16(srcU[i], sbase + slot * B1_STAGE + 49152 + (wid * 2 + i) * 1024); srcU[i] += 64 * F_ * 2; }
#pragma unroll
        for (int i = 0; i < 4; ++i) { dma16(srcA[i], sbase + slot * B1_STAGE + (wid * 4 + i) * 1024); srcA[i] += 128; }
    };
    const int wm = wid >> 2, wn = wid & 3;
    const int fr = lane & 15, q = lane >> 4, qq = fr >> 2, pp = lane & 3;
    int offM[2];
#pragma unroll
    for (int kk = 0; kk < 2; ++kk) offM[kk] = (wm * 128 + fr) * 128 + (((kk * 4 + q) ^ (fr >> 1)) * 16);
    int offN[2];
    const int f = qq | ((q & 1) << 2);
#pragma unroll
    for (int ni = 0; ni < 2; ++ni) offN[ni] = 32768 + (8 * q + qq) * 256 + 32 * ((wn * 2 + ni) ^ f) + 8 * pp;
    f32x4 accG[2][8], accU[2][8];
#pragma unroll
    for (int a = 0; a < 2; ++a)
#pragma unroll
        for (int b = 0; b < 8; ++b) { accG[a][b] = (f32x4){0.f, 0.f, 0.f, 0.f}; accU[a][b] = (f32x4){0.f, 0.f, 0.f, 0.f}; }
    issue(0);
#pragma unroll 1
    for (int t2 = 0; t2 < B1_NT; t2 += 2) {
#pragma unroll
        for (int s = 0; s < 2; ++s) {
            WAIT_VM(0); BAR();
            if (t2 + s + 1 < B1_NT) issue(s ^ 1);
            lds_ptr sp = smem + s * B1_STAGE;
#pragma unroll
            for (int kk = 0; kk < 2; ++kk) {
                bf16x8 G[2], U[2], M[8];
#pragma unroll
                for (int i = 0; i < 2; ++i) { G[i] = lds_tr_frag(sp + offN[i] + kk * 8192, 1024); U[i] = lds_tr_frag(sp + offN[i] + 16384 + kk * 8192, 1024); }
#pragma unroll
                for (int i = 0; i < 8; ++i) M[i] = lds_row_frag(sp + offM[kk] + i * 2048);
#pragma unroll
                for (int mi = 0; mi < 8; ++mi)
#pragma unroll
                    for (int ni = 0; ni < 2; ++ni) {
                        accG[ni][mi] = mfma16(G[ni], M[mi], accG[ni][mi]);
                        accU[ni][mi] = mfma16(U[ni], M[mi], accU[ni][mi]);
                    }
            }
            WAIT_LGKM0();
        }
    }
    bf16_t* zb = p.z + (size_t)(row0 + wm * 128 + fr) * ZLD + 1024 + f0 + wn * 32 + 4 * q;
#pragma unroll
    for (int mi = 0; mi < 8; ++mi)
#pragma unroll
        for (int ni = 0; ni < 2; ++ni) {
            const f32x4 gv = accG[ni][mi], uv = accU[ni][mi];
            u32x2 w;
            w.x = pk_bf16(sigmoidf_(gv[0]) * uv[0], sigmoidf_(gv[1]) * uv[1]);
            w.y = pk_bf16(sigmoidf_(gv[2]) * uv[2], sigmoidf_(gv[3]) * uv[3]);
            *(u32x2*)(zb + (size_t)mi * 16 * ZLD + ni * 16) = w;
        }
}

__device__ __forceinline__ float bf2f(short v) { return __uint_as_float(((unsigned)(unsigned short)v) << 16); }
__device__ __forceinline__ void gate_tile(const Params& p, int row0, int wid, int lane) {
    const bf16x8 w = *(const bf16x8*)(p.gw + lane * 8);
    bf16x8 xv[16];
#pragma unroll
    for (int i = 0; i < 16; ++i) xv[i] = *(const bf16x8*)(p.x + (size_t)(row0 + wid * 16 + i) * D_ + lane * 8);
#pragma unroll
    for (int i = 0; i < 16; ++i) {
        float s = 0.f;
#pragma unroll
        for (int j = 0; j < 8; ++j) s += bf2f(xv[i][j]) * bf2f(w[j]);
#pragma unroll
        for (int o = 32; o > 0; o >>= 1) s += __shfl_xor(s, o);
        if (lane == 0) p.g[row0 + wid * 16 + i] = sigmoidf_(s);
    }
}

__global__ __launch_bounds__(512, 2) void stage1_kernel(Params p) {
    extern __shared__ __attribute__((aligned(16))) char smem_g[];
    lds_ptr smem = (lds_ptr)smem_g;
    const int lane = threadIdx.x & 63;
    const int wid = __builtin_amdgcn_readfirstlane(threadIdx.x >> 6);
    const int xcd = blockIdx.x & 7, idx = blockIdx.x >> 3;
    if (idx < 16) {
        const int mat = (idx >> 3) & 1, half = (idx >> 2) & 1, ct = idx & 3;
        a1_tile(p, smem, mat, xcd * 256 + half * 128, ct * 128, wid, lane);
    } else if (idx < 30) {
        b1_tile(p, smem, xcd * 256, (idx - 16) * 128, wid, lane);
    } else {
        gate_tile(p, xcd * 256 + (idx - 30) * 128, wid, lane);
    }
}

constexpr int S2_ST = 4, S2_STAGE = 32768, S2_NT = 22, S2_NT1 = 8;
__global__ __launch_bounds__(512, 2) void stage2_kernel(Params p) {
    extern __shared__ __attribute__((aligned(16))) char smem_g[];
    lds_ptr smem = (lds_ptr)smem_g;
    const unsigned sbase = (unsigned)(size_t)smem;
    const int lane = threadIdx.x & 63;
    const int wid = __builtin_amdgcn_readfirstlane(threadIdx.x >> 6);
    const int xcd = blockIdx.x & 7, idx = blockIdx.x >> 3;
    const int row0 = xcd * 256 + (idx >> 3) * 64, col0 = (idx & 7) * 64;
    if (wid >= 4) {
        const int lw = wid - 4;
        const char* srcA[4]; const char* srcH[4]; const char* srcW[4];
#pragma unroll
        for (int i = 0; i < 4; ++i) {
            const int piece = lw * 4 + i, sub = piece >> 3;
            const int r = (piece & 7) * 8 + (lane >> 3), c = (lane & 7) ^ ((r >> 1) & 7);
            srcA[i] = (const char*)(p.z + (size_t)(row0 + r) * ZLD + sub * 64) + c * 16;
            srcH[i] = (const char*)(p.hr + (size_t)(col0 + r) * D_ + sub * 64) + c * 16;
            const int k = r, cc = (lane & 7) ^ swz_k128(k);
            srcW[i] = (const char*)(p.wd + (size_t)(sub * 64 + k) * D_ + col0) + cc * 16;
        }
        const long dHi = (const char*)p.hi - (const char*)p.hr - 1024;
        auto issue = [&](int t, int slot) {
            const unsigned sb = sbase + slot * S2_STAGE + lw * 4096;
            if (t < S2_NT1) {
#pragma unroll
                for (int i = 0; i < 4; ++i) { dma16(srcH[i], sb + 16384 + i * 1024); srcH[i] += (t == 3) ? (256 + dHi) : 256; }
            } else {
#pragma unroll
                for (int i = 0; i < 4; ++i) { dma16(srcW[i], sb + 16384 + i * 1024); srcW[i] += 128 * D_ * 2; }
            }
#pragma unroll
            for (int i = 0; i < 4; ++i) { dma16(srcA[i], sb + i * 1024); srcA[i] += 256; }
        };
#pragma unroll
        for (int s = 0; s < S2_ST; ++s) issue(s, s);
        WAIT_VM((S2_ST - 1) * 8); BAR();
        for (int t = 0; t < S2_NT - S2_ST; ++t) {
            WAIT_VM((S2_ST - 2) * 8); BAR();
            issue(t + S2_ST, t % S2_ST);
        }
        WAIT_VM(16); BAR();
        WAIT_VM(8); BAR();
        WAIT_VM(0); BAR();
        BAR();
        return;
    }
    const int wm = wid >> 1, wn = wid & 1;
    const int fr = lane & 15, q = lane >> 4, qq = fr >> 2, pp = lane & 3;
    int offM[2], offH[2], offW[2];
#pragma unroll
    for (int kk = 0; kk < 2; ++kk) {
        offM[kk] = (wm * 32 + fr) * 128 + (((kk * 4 + q) ^ (fr >> 1)) * 16);
        offH[kk] = 16384 + (wn * 32 + fr) * 128 + (((kk * 4 + q) ^ (fr >> 1)) * 16);
    }
    const int f = (qq >> 1) | ((q & 1) << 1);
#pragma unroll
    for (int ni = 0; ni < 2; ++ni) offW[ni] = 16384 + (8 * q + qq) * 128 + 32 * ((wn * 2 + ni) ^ f) + 8 * pp;
    f32x4 acc1[2][2], acc2[2][2];
#pragma unroll
    for (int a = 0; a < 2; ++a)
#pragma unroll
        for (int b = 0; b < 2; ++b) { acc1[a][b] = (f32x4){0.f, 0.f, 0.f, 0.f}; acc2[a][b] = (f32x4){0.f, 0.f, 0.f, 0.f}; }
    bf16x8 M0[2][2], N0[2][2], M1[2][2], N1[2][2];
    auto rd = [&](bf16x8(&M)[2][2], bf16x8(&N)[2][2], int slot, int sub, bool ph1) {
        lds_ptr s = smem + slot * S2_STAGE + sub * 8192;
#pragma unroll
        for (int kk = 0; kk < 2; ++kk)
#pragma unroll
            for (int i = 0; i < 2; ++i) {
                if (ph1) N[kk][i] = lds_row_frag(s + offH[kk] + i * 2048);
                else N[kk][i] = lds_tr_frag(s + offW[i] + kk * 4096, 512);
                M[kk][i] = lds_row_frag(s + offM[kk] + i * 2048);
            }
    };
    auto mm = [&](bf16x8(&M)[2][2], bf16x8(&N)[2][2], f32x4(&acc)[2][2]) {
#pragma unroll
        for (int kk = 0; kk < 2; ++kk)
#pragma unroll
            for (int mi = 0; mi < 2; ++mi)
#pragma unroll
                for (int ni = 0; ni < 2; ++ni) acc[ni][mi] = mfma16(N[kk][ni], M[kk][mi], acc[ni][mi]);
    };
    BAR();
    rd(M0, N0, 0, 0, true);
#pragma unroll
    for (int t = 0; t < S2_NT1; ++t) {
        rd(M1, N1, t % S2_ST, 1, true);
        mm(M0, N0, acc1);
        WAIT_LGKM0(); BAR();
        rd(M0, N0, (t + 1) % S2_ST, 0, t + 1 < S2_NT1);
        mm(M1, N1, acc1);
    }
#pragma unroll
    for (int t = S2_NT1; t < S2_NT; ++t) {
        rd(M1, N1, t % S2_ST, 1, false);
        mm(M0, N0, acc2);
        WAIT_LGKM0(); BAR();
        if (t + 1 < S2_NT) rd(M0, N0, (t + 1) % S2_ST, 0, false);
        mm(M1, N1, acc2);
    }
#pragma unroll
    for (int mi = 0; mi < 2; ++mi) {
        const int row = row0 + wm * 32 + mi * 16 + fr;
        const float g = p.g[row], g1 = 1.f - g;
#pragma unroll
        for (int ni = 0; ni < 2; ++ni) {
            const f32x4 v = acc1[ni][mi] * g + acc2[ni][mi] * g1;
            u32x2 w; w.x = pk_bf16(v[0], v[1]); w.y = pk_bf16(v[2], v[3]);
            *(u32x2*)(p.out + (size_t)row * D_ + col0 + wn * 32 + ni * 16 + 4 * q) = w;
        }
    }
}

extern "C" void kernel_launch(void* const* d_in, const int* in_sizes, int n_in, void* d_out, int out_size, void* d_ws,
                              size_t ws_size, hipStream_t stream) {
    Params p;
    p.x = (const bf16_t*)d_in[0]; p.cr = (const bf16_t*)d_in[1]; p.ci = (const bf16_t*)d_in[2];
    p.hr = (const bf16_t*)d_in[3]; p.hi = (const bf16_t*)d_in[4];
    p.wg = (const bf16_t*)d_in[6]; p.wu = (const bf16_t*)d_in[7]; p.wd = (const bf16_t*)d_in[8]; p.gw = (const bf16_t*)d_in[9];
    p.z = (bf16_t*)d_ws; p.g = (float*)((char*)d_ws + (size_t)S_ * ZLD * 2); p.out = (bf16_t*)d_out;
    (void)hipFuncSetAttribute((const void*)stage1_kernel, hipFuncAttributeMaxDynamicSharedMemorySize, 131072);
    (void)hipFuncSetAttribute((const void*)stage2_kernel, hipFuncAttributeMaxDynamicSharedMemorySize, 131072);
    stage1_kernel<<<256, 512, 131072, stream>>>(p);
    stage2_kernel<<<256, 512, 131072, stream>>>(p);
}
```

```cpp
#include <hip/hip_runtime.h>
#include <hip/hip_bf16.h>

#define LAS __attribute__((address_space(3)))
typedef unsigned short bf16_t;
typedef short bf16x8 __attribute__((ext_vector_type(8)));
typedef short bf16x4 __attribute__((ext_vector_type(4)));
typedef float f32x4 __attribute__((ext_vector_type(4)));
typedef unsigned u32x4 __attribute__((ext_vector_type(4)));
typedef unsigned u32x2 __attribute__((ext_vector_type(2)));
typedef LAS char* lds_ptr;

constexpr int S_ = 2048, D_ = 512, F_ = 1792, ZLD = 2816;

#define WAIT_VM(n) asm volatile("s_waitcnt vmcnt(%0)" ::"i"(n) : "memory")
#define WAIT_LGKM0() asm volatile("s_waitcnt lgkmcnt(0)" ::: "memory")
#define BAR() do { asm volatile("" ::: "memory"); __builtin_amdgcn_s_barrier(); asm volatile("" ::: "memory"); } while (0)

__device__ __forceinline__ void dma16(const char* gsrc, unsigned ldsaddr) {
    asm volatile("s_mov_b32 m0, %1\n\ts_nop 0\n\tglobal_load_lds_dwordx4 %0, off" ::"v"(gsrc), "s"(ldsaddr) : "memory");
}
__device__ __forceinline__ bf16x8 lds_row_frag(lds_ptr p) { return *(LAS bf16x8*)p; }
__device__ __forceinline__ bf16x8 lds_tr_frag(lds_ptr p, int off2) {
    bf16x4 a = __builtin_amdgcn_ds_read_tr16_b64_v4i16((LAS bf16x4*)p);
    bf16x4 b = __builtin_amdgcn_ds_read_tr16_b64_v4i16((LAS bf16x4*)(p + off2));
    return __builtin_shufflevector(a, b, 0, 1, 2, 3, 4, 5, 6, 7);
}
__device__ __forceinline__ f32x4 mfma16(bf16x8 a, bf16x8 b, f32x4 c) { return __builtin_amdgcn_mfma_f32_16x16x32_bf16(a, b, c, 0, 0, 0); }
__device__ __forceinline__ unsigned pk_bf16(float lo, float hi) {
    unsigned r; asm("v_cvt_pk_bf16_f32 %0, %1, %2" : "=v"(r) : "v"(lo), "v"(hi)); return r;
}
__device__ __forceinline__ float sigmoidf_(float v) { return __builtin_amdgcn_rcpf(1.0f + __builtin_amdgcn_exp2f(-1.44269504088896f * v)); }

__device__ __forceinline__ int swz_k256(int k) { return (((k & 3) | (((k >> 3) & 1) << 2)) << 1); }
__device__ __forceinline__ int swz_k128(int k) { return ((((k >> 1) & 1) | (((k >> 3) & 1) << 1)) << 1); }

#define STAMP_DECL
#define STAMP(i) do {} while (0)
#define STAMP_FLUSH(kidx) do {} while (0)
struct Params {
    const bf16_t* x; const bf16_t* cr; const bf16_t* ci; const bf16_t* hr; const bf16_t* hi;
    const bf16_t* wg; const bf16_t* wu; const bf16_t* wd; const bf16_t* gw;
    bf16_t* z; float* g; bf16_t* out;
};

constexpr int A1_ST = 4, A1_STAGE = 32768, A1_NT = 32;
__device__ __forceinline__ void a1_tile(const Params& p, lds_ptr smem, int mat, int row0, int col0, int wid, int lane) {
    const unsigned sbase = (unsigned)(size_t)smem;
    STAMP_DECL; STAMP(0);
    if (wid >= 4) {
        const int lw = wid - 4;
        const bf16_t* C = mat ? p.ci : p.cr;
        const char* srcA[4]; const char* srcB[4];
#pragma unroll
        for (int i = 0; i < 4; ++i) {
            const int piece = lw * 4 + i;
            const int r = piece * 8 + (lane >> 3), c = (lane & 7) ^ ((r >> 1) & 7);
            srcA[i] = (const char*)(C + (size_t)(row0 + r) * S_) + c * 16;
            const int k = piece * 4 + (lane >> 4), cc = (lane & 15) ^ swz_k256(k);
            srcB[i] = (const char*)(p.x + (size_t)k * D_ + col0) + cc * 16;
        }
        auto issue = [&](int slot) {
#pragma unroll
            for (int i = 0; i < 4; ++i) { dma16(srcA[i], sbase + slot * A1_STAGE + (lw * 4 + i) * 1024); srcA[i] += 128; }
#pragma unroll
            for (int i = 0; i < 4; ++i) { dma16(srcB[i], sbase + slot * A1_STAGE + 16384 + (lw * 4 + i) * 1024); srcB[i] += 64 * D_ * 2; }
        };
#pragma unroll
        for (int s = 0; s < A1_ST; ++s) issue(s);
        WAIT_VM((A1_ST - 1) * 8); BAR();
        for (int t = 0; t < A1_NT - A1_ST; ++t) {
            WAIT_VM((A1_ST - 2) * 8); BAR();
            issue(t % A1_ST);
        }
        WAIT_VM(16); BAR();
        WAIT_VM(8); BAR();
        WAIT_VM(0); BAR();
        BAR();
        STAMP_FLUSH(0);
        return;
    }
    const int wm = wid >> 1, wn = wid & 1;
    const int fr = lane & 15, q = lane >> 4, qq = fr >> 2, pp = lane & 3;
    int offM[2];
#pragma unroll
    for (int kk = 0; kk < 2; ++kk) offM[kk] = (wm * 64 + fr) * 128 + (((kk * 4 + q) ^ (fr >> 1)) * 16);
    int offN[4];
    const int f = qq | ((q & 1) << 2);
#pragma unroll
    for (int ni = 0; ni < 4; ++ni) offN[ni] = 16384 + (8 * q + qq) * 256 + 32 * ((wn * 4 + ni) ^ f) + 8 * pp;
    f32x4 acc[4][4];
#pragma unroll
    for (int a = 0; a < 4; ++a)
#pragma unroll
        for (int b = 0; b < 4; ++b) acc[a][b] = (f32x4){0.f, 0.f, 0.f, 0.f};
    bf16x8 M0[4], N0[4], M1[4], N1[4];
    auto rd = [&](bf16x8(&M)[4], bf16x8(&N)[4], int slot, int kk) {
        lds_ptr s = smem + slot * A1_STAGE;
#pragma unroll
        for (int i = 0; i < 4; ++i) N[i] = lds_tr_frag(s + offN[i] + kk * 8192, 1024);
#pragma unroll
        for (int i = 0; i < 4; ++i) M[i] = lds_row_frag(s + offM[kk] + i * 2048);
    };
    auto mm = [&](bf16x8(&M)[4], bf16x8(&N)[4]) {
#pragma unroll
        for (int mi = 0; mi < 4; ++mi)
#pragma unroll
            for (int ni = 0; ni < 4; ++ni) acc[ni][mi] = mfma16(N[ni], M[mi], acc[ni][mi]);
    };
    BAR();
    STAMP(1);
    rd(M0, N0, 0, 0);
#pragma unroll 1
    for (int t4 = 0; t4 < A1_NT; t4 += A1_ST) {
#pragma unroll
        for (int s = 0; s < A1_ST; ++s) {
            rd(M1, N1, s, 1);
            mm(M0, N0);
            WAIT_LGKM0(); BAR();
            if (s + 1 < A1_ST || t4 + A1_ST < A1_NT) rd(M0, N0, (s + 1) % A1_ST, 0);
            mm(M1, N1);
        }
    }
    STAMP(2);
    const float sg = mat ? -1.f : 1.f;
    bf16_t* zb = p.z + (size_t)(row0 + wm * 64 + fr) * ZLD + mat * 512 + col0 + wn * 64 + 4 * q;
#pragma unroll
    for (int mi = 0; mi < 4; ++mi)
#pragma unroll
        for (int ni = 0; ni < 4; ++ni) {
            const f32x4 v = acc[ni][mi] * sg;
            u32x2 w; w.x = pk_bf16(v[0], v[1]); w.y = pk_bf16(v[2], v[3]);
            *(u32x2*)(zb + (size_t)mi * 16 * ZLD + ni * 16) = w;
        }
    STAMP_FLUSH(0);
}

constexpr int B1_STAGE = 65536, B1_NT = 8;
__device__ __forceinline__ void b1_tile(const Params& p, lds_ptr smem, int row0, int f0, int wid, int lane) {
    const unsigned sbase = (unsigned)(size_t)smem;
    STAMP_DECL; STAMP(0);
    const char* srcA[4]; const char* srcG[2]; const char* srcU[2];
#pragma unroll
    for (int i = 0; i < 4; ++i) {
        const int piece = wid * 4 + i;
        const int r = piece * 8 + (lane >> 3), c = (lane & 7) ^ ((r >> 1) & 7);
        srcA[i] = (const char*)(p.x + (size_t)(row0 + r) * D_) + c * 16;
    }
#pragma unroll
    for (int i = 0; i < 2; ++i) {
        const int piece = wid * 2 + i;
        const int k = piece * 4 + (lane >> 4), cc = (lane & 15) ^ swz_k256(k);
        srcG[i] = (const char*)(p.wg + (size_t)k * F_ + f0) + cc * 16;
        srcU[i] = (const char*)(p.wu + (size_t)k * F_ + f0) + cc * 16;
    }
    auto issue = [&](int slot) {
#pragma unroll
        for (int i = 0; i < 2; ++i) { dma16(srcG[i], sbase + slot * B1_STAGE + 32768 + (wid * 2 + i) * 1024); srcG[i] += 64 * F_ * 2; }
#pragma unroll
        for (int i = 0; i < 2; ++i) { dma16(srcU[i], sbase + slot * B1_STAGE + 49152 + (wid * 2 + i) * 1024); srcU[i] += 64 * F_ * 2; }
#pragma unroll
        for (int i = 0; i < 4; ++i) { dma16(srcA[i], sbase + slot * B1_STAGE + (wid * 4 + i) * 1024); srcA[i] += 128; }
    };
    const int wm = wid >> 2, wn = wid & 3;
    const int fr = lane & 15, q = lane >> 4, qq = fr >> 2, pp = lane & 3;
    int offM[2];
#pragma unroll
    for (int kk = 0; kk < 2; ++kk) offM[kk] = (wm * 128 + fr) * 128 + (((kk * 4 + q) ^ (fr >> 1)) * 16);
    int offN[2];
    const int f = qq | ((q & 1) << 2);
#pragma unroll
    for (int ni = 0; ni < 2; ++ni) offN[ni] = 32768 + (8 * q + qq) * 256 + 32 * ((wn * 2 + ni) ^ f) + 8 * pp;
    f32x4 accG[2][8], accU[2][8];
#pragma unroll
    for (int a = 0; a < 2; ++a)
#pragma unroll
        for (int b = 0; b < 8; ++b) { accG[a][b] = (f32x4){0.f, 0.f, 0.f, 0.f}; accU[a][b] = (f32x4){0.f, 0.f, 0.f, 0.f}; }
    issue(0);
#pragma unroll 1
    for (int t2 = 0; t2 < B1_NT; t2 += 2) {
#pragma unroll
        for (int s = 0; s < 2; ++s) {
            WAIT_VM(0); BAR();
            if (t2 + s == 0) STAMP(1);
            if (t2 + s + 1 < B1_NT) issue(s ^ 1);
            lds_ptr sp = smem + s * B1_STAGE;
#pragma unroll
            for (int kk = 0; kk < 2; ++kk) {
                bf16x8 G[2], U[2], M[8];
#pragma unroll
                for (int i = 0; i < 2; ++i) { G[i] = lds_tr_frag(sp + offN[i] + kk * 8192, 1024); U[i] = lds_tr_frag(sp + offN[i] + 16384 + kk * 8192, 1024); }
#pragma unroll
                for (int i = 0; i < 8; ++i) M[i] = lds_row_frag(sp + offM[kk] + i * 2048);
#pragma unroll
                for (int mi = 0; mi < 8; ++mi)
#pragma unroll
                    for (int ni = 0; ni < 2; ++ni) {
                        accG[ni][mi] = mfma16(G[ni], M[mi], accG[ni][mi]);
                        accU[ni][mi] = mfma16(U[ni], M[mi], accU[ni][mi]);
                    }
            }
            WAIT_LGKM0();
        }
    }
    STAMP(2);
    bf16_t* zb = p.z + (size_t)(row0 + wm * 128 + fr) * ZLD + 1024 + f0 + wn * 32 + 4 * q;
#pragma unroll
    for (int mi = 0; mi < 8; ++mi)
#pragma unroll
        for (int ni = 0; ni < 2; ++ni) {
            const f32x4 gv = accG[ni][mi], uv = accU[ni][mi];
            u32x2 w;
            w.x = pk_bf16(sigmoidf_(gv[0]) * uv[0], sigmoidf_(gv[1]) * uv[1]);
            w.y = pk_bf16(sigmoidf_(gv[2]) * uv[2], sigmoidf_(gv[3]) * uv[3]);
            *(u32x2*)(zb + (size_t)mi * 16 * ZLD + ni * 16) = w;
        }
    STAMP_FLUSH(0);
}

__device__ __forceinline__ float bf2f(short v) { return __uint_as_float(((unsigned)(unsigned short)v) << 16); }
__device__ __forceinline__ void gate_tile(const Params& p, int row0, int wid, int lane) {
    STAMP_DECL; STAMP(0);
    const bf16x8 w = *(const bf16x8*)(p.gw + lane * 8);
    bf16x8 xv[16];
#pragma unroll
    for (int i = 0; i < 16; ++i) xv[i] = *(const bf16x8*)(p.x + (size_t)(row0 + wid * 16 + i) * D_ + lane * 8);
#pragma unroll
    for (int i = 0; i < 16; ++i) {
        float s = 0.f;
#pragma unroll
        for (int j = 0; j < 8; ++j) s += bf2f(xv[i][j]) * bf2f(w[j]);
#pragma unroll
        for (int o = 32; o > 0; o >>= 1) s += __shfl_xor(s, o);
        if (lane == 0) p.g[row0 + wid * 16 + i] = sigmoidf_(s);
    }
    STAMP_FLUSH(0);
}

__global__ __launch_bounds__(512, 2) void stage1_kernel(Params p) {
    extern __shared__ __attribute__((aligned(16))) char smem_g[];
    lds_ptr smem = (lds_ptr)smem_g;
    const int lane = threadIdx.x & 63;
    const int wid = __builtin_amdgcn_readfirstlane(threadIdx.x >> 6);
    const int xcd = blockIdx.x & 7, idx = blockIdx.x >> 3;
    if (idx < 16) {
        const int mat = (idx >> 3) & 1, half = (idx >> 2) & 1, ct = idx & 3;
        a1_tile(p, smem, mat, xcd * 256 + half * 128, ct * 128, wid, lane);
    } else if (idx < 30) {
        b1_tile(p, smem, xcd * 256, (idx - 16) * 128, wid, lane);
    } else {
        gate_tile(p, xcd * 256 + (idx - 30) * 128, wid, lane);
    }
}

constexpr int S2_ST = 4, S2_STAGE = 32768, S2_NT = 22, S2_NT1 = 8;
__global__ __launch_bounds__(512, 2) void stage2_kernel(Params p) {
    extern __shared__ __attribute__((aligned(16))) char smem_g[];
    lds_ptr smem = (lds_ptr)smem_g;
    const unsigned sbase = (unsigned)(size_t)smem;
    const int lane = threadIdx.x & 63;
    const int wid = __builtin_amdgcn_readfirstlane(threadIdx.x >> 6);
    const int xcd = blockIdx.x & 7, idx = blockIdx.x >> 3;
    const int row0 = xcd * 256 + (idx >> 3) * 64, col0 = (idx & 7) * 64;
    STAMP_DECL; STAMP(0);
    if (wid >= 4) {
        const int lw = wid - 4;
        const char* srcA[4]; const char* srcH[4]; const char* srcW[4];
#pragma unroll
        for (int i = 0; i < 4; ++i) {
            const int piece = lw * 4 + i, sub = piece >> 3;
            const int r = (piece & 7) * 8 + (lane >> 3), c = (lane & 7) ^ ((r >> 1) & 7);
            srcA[i] = (const char*)(p.z + (size_t)(row0 + r) * ZLD + sub * 64) + c * 16;
            srcH[i] = (const char*)(p.hr + (size_t)(col0 + r) * D_ + sub * 64) + c * 16;
            const int k = r, cc = (lane & 7) ^ swz_k128(k);
            srcW[i] = (const char*)(p.wd + (size_t)(sub * 64 + k) * D_ + col0) + cc * 16;
        }
        const long dHi = (const char*)p.hi - (const char*)p.hr - 1024;
        auto issue = [&](int t, int slot) {
            const unsigned sb = sbase + slot * S2_STAGE + lw * 4096;
            if (t < S2_NT1) {
#pragma unroll
                for (int i = 0; i < 4; ++i) { dma16(srcH[i], sb + 16384 + i * 1024); srcH[i] += (t == 3) ? (256 + dHi) : 256; }
            } else {
#pragma unroll
                for (int i = 0; i < 4; ++i) { dma16(srcW[i], sb + 16384 + i * 1024); srcW[i] += 128 * D_ * 2; }
            }
#pragma unroll
            for (int i = 0; i < 4; ++i) { dma16(srcA[i], sb + i * 1024); srcA[i] += 256; }
        };
#pragma unroll
        for (int s = 0; s < S2_ST; ++s) issue(s, s);
        WAIT_VM((S2_ST - 1) * 8); BAR();
        for (int t = 0; t < S2_NT - S2_ST; ++t) {
            WAIT_VM((S2_ST - 2) * 8); BAR();
            issue(t + S2_ST, t % S2_ST);
        }
        WAIT_VM(16); BAR();
        WAIT_VM(8); BAR();
        WAIT_VM(0); BAR();
        BAR();
        STAMP_FLUSH(1);
        return;
    }
    const int wm = wid >> 1, wn = wid & 1;
    const int fr = lane & 15, q = lane >> 4, qq = fr >> 2, pp = lane & 3;
    int offM[2], offH[2], offW[2];
#pragma unroll
    for (int kk = 0; kk < 2; ++kk) {
        offM[kk] = (wm * 32 + fr) * 128 + (((kk * 4 + q) ^ (fr >> 1)) * 16);
        offH[kk] = 16384 + (wn * 32 + fr) * 128 + (((kk * 4 + q) ^ (fr >> 1)) * 16);
    }
    const int f = (qq >> 1) | ((q & 1) << 1);
#pragma unroll
    for (int ni = 0; ni < 2; ++ni) offW[ni] = 16384 + (8 * q + qq) * 128 + 32 * ((wn * 2 + ni) ^ f) + 8 * pp;
    f32x4 acc1[2][2], acc2[2][2];
#pragma unroll
    for (int a = 0; a < 2; ++a)
#pragma unroll
        for (int b = 0; b < 2; ++b) { acc1[a][b] = (f32x4){0.f, 0.f, 0.f, 0.f}; acc2[a][b] = (f32x4){0.f, 0.f, 0.f, 0.f}; }
    bf16x8 M0[2][2], N0[2][2], M1[2][2], N1[2][2];
    auto rd = [&](bf16x8(&M)[2][2], bf16x8(&N)[2][2], int slot, int sub, bool ph1) {
        lds_ptr s = smem + slot * S2_STAGE + sub * 8192;
#pragma unroll
        for (int kk = 0; kk < 2; ++kk)
#pragma unroll
            for (int i = 0; i < 2; ++i) {
                if (ph1) N[kk][i] = lds_row_frag(s + offH[kk] + i * 2048);
                else N[kk][i] = lds_tr_frag(s + offW[i] + kk * 4096, 512);
                M[kk][i] = lds_row_frag(s + offM[kk] + i * 2048);
            }
    };
    auto mm = [&](bf16x8(&M)[2][2], bf16x8(&N)[2][2], f32x4(&acc)[2][2]) {
#pragma unroll
        for (int kk = 0; kk < 2; ++kk)
#pragma unroll
            for (int mi = 0; mi < 2; ++mi)
#pragma unroll
                for (int ni = 0; ni < 2; ++ni) acc[ni][mi] = mfma16(N[kk][ni], M[kk][mi], acc[ni][mi]);
    };
    BAR();
    STAMP(1);
    rd(M0, N0, 0, 0, true);
#pragma unroll
    for (int t = 0; t < S2_NT1; ++t) {
        rd(M1, N1, t % S2_ST, 1, true);
        mm(M0, N0, acc1);
        WAIT_LGKM0(); BAR();
        rd(M0, N0, (t + 1) % S2_ST, 0, t + 1 < S2_NT1);
        mm(M1, N1, acc1);
    }
#pragma unroll
    for (int t = S2_NT1; t < S2_NT; ++t) {
        rd(M1, N1, t % S2_ST, 1, false);
        mm(M0, N0, acc2);
        WAIT_LGKM0(); BAR();
        if (t + 1 < S2_NT) rd(M0, N0, (t + 1) % S2_ST, 0, false);
        mm(M1, N1, acc2);
    }
    STAMP(2);
#pragma unroll
    for (int mi = 0; mi < 2; ++mi) {
        const int row = row0 + wm * 32 + mi * 16 + fr;
        const float g = p.g[row], g1 = 1.f - g;
#pragma unroll
        for (int ni = 0; ni < 2; ++ni) {
            const f32x4 v = acc1[ni][mi] * g + acc2[ni][mi] * g1;
            u32x2 w; w.x = pk_bf16(v[0], v[1]); w.y = pk_bf16(v[2], v[3]);
            *(u32x2*)(p.out + (size_t)row * D_ + col0 + wn * 32 + ni * 16 + 4 * q) = w;
        }
    }
    STAMP_FLUSH(1);
}

extern "C" void kernel_launch(void* const* d_in, const int* in_sizes, int n_in, void* d_out, int out_size, void* d_ws,
                              size_t ws_size, hipStream_t stream) {
    Params p;
    p.x = (const bf16_t*)d_in[0]; p.cr = (const bf16_t*)d_in[1]; p.ci = (const bf16_t*)d_in[2];
    p.hr = (const bf16_t*)d_in[3]; p.hi = (const bf16_t*)d_in[4];
    p.wg = (const bf16_t*)d_in[6]; p.wu = (const bf16_t*)d_in[7]; p.wd = (const bf16_t*)d_in[8]; p.gw = (const bf16_t*)d_in[9];
    p.z = (bf16_t*)d_ws; p.g = (float*)((char*)d_ws + (size_t)S_ * ZLD * 2); p.out = (bf16_t*)d_out;
    (void)hipFuncSetAttribute((const void*)stage1_kernel, hipFuncAttributeMaxDynamicSharedMemorySize, 131072);
    (void)hipFuncSetAttribute((const void*)stage2_kernel, hipFuncAttributeMaxDynamicSharedMemorySize, 131072);
    stage1_kernel<<<256, 512, 131072, stream>>>(p);
    stage2_kernel<<<256, 512, 131072, stream>>>(p);
}
```

```cpp
#include <hip/hip_runtime.h>
#include <hip/hip_bf16.h>
#include <type_traits>

#define IPF2 10112
#define IPFS 0
#define TLBT 1
#define LAS __attribute__((address_space(3)))
typedef unsigned short bf16_t;
typedef short bf16x8 __attribute__((ext_vector_type(8)));
typedef short bf16x4 __attribute__((ext_vector_type(4)));
typedef float f32x4 __attribute__((ext_vector_type(4)));
typedef unsigned u32x4 __attribute__((ext_vector_type(4)));
typedef unsigned u32x2 __attribute__((ext_vector_type(2)));
typedef LAS char* lds_ptr;

constexpr int S_ = 2048, D_ = 512, F_ = 1792;

#define WAIT_VM(n) asm volatile("s_waitcnt vmcnt(%0)" ::"i"(n) : "memory")
#define WAIT_LGKM0() asm volatile("s_waitcnt lgkmcnt(0)" ::: "memory")
#define BAR() do { asm volatile("" ::: "memory"); __builtin_amdgcn_s_barrier(); asm volatile("" ::: "memory"); } while (0)
#define SB() __builtin_amdgcn_sched_barrier(0)
#define ADV(a, d) do { (a) += (d); asm volatile("" : "+v"(a)); } while (0)

#define asm_dma asm volatile
__device__ __forceinline__ void dma16(const char* sbase, unsigned voff, unsigned ldsaddr) {
    asm_dma("s_mov_b32 m0, %2\n\ts_nop 0\n\tglobal_load_lds_dwordx4 %0, %1" ::"v"(voff), "s"(sbase), "s"(ldsaddr) : "memory");
}
__device__ __forceinline__ void dma16nt(const char* sbase, unsigned voff, unsigned ldsaddr) {
    asm_dma("s_mov_b32 m0, %2\n\ts_nop 0\n\tglobal_load_lds_dwordx4 %0, %1" ::"v"(voff), "s"(sbase), "s"(ldsaddr) : "memory");
}
__device__ __forceinline__ void dma16x(const char* sbase, unsigned voff, unsigned ldsaddr) {
    asm_dma("s_mov_b32 m0, %2\n\ts_nop 0\n\tglobal_load_lds_dwordx4 %0, %1 nt" ::"v"(voff), "s"(sbase), "s"(ldsaddr) : "memory");
}
__device__ __forceinline__ void dma16v(const char* gsrc, unsigned ldsaddr) {
    asm_dma("s_mov_b32 m0, %1\n\ts_nop 0\n\tglobal_load_lds_dwordx4 %0, off" ::"v"(gsrc), "s"(ldsaddr) : "memory");
}
template <int NT> __device__ __forceinline__ void pf_line(const char* gsrc, unsigned ldsaddr) {
    if (NT) asm volatile("s_mov_b32 m0, %1\n\ts_nop 0\n\tglobal_load_lds_dword %0, off nt" ::"v"(gsrc), "s"(ldsaddr) : "memory");
    else asm volatile("s_mov_b32 m0, %1\n\ts_nop 0\n\tglobal_load_lds_dword %0, off" ::"v"(gsrc), "s"(ldsaddr) : "memory");
}
#define PFO_NT 1
__device__ __forceinline__ void st16(void* g, u32x4 v) {
    asm volatile("global_store_dwordx4 %0, %1, off sc1\n\ts_nop 1" ::"v"(g), "v"(v) : "memory");
}
__device__ __forceinline__ void st8(void* g, u32x2 v) {
    asm volatile("global_store_dwordx2 %0, %1, off sc1\n\ts_nop 1" ::"v"(g), "v"(v) : "memory");
}
__device__ __forceinline__ bf16x8 lds_row_frag(unsigned a) { return *(LAS bf16x8*)a; }
__device__ __forceinline__ bf16x4 lds_tr(unsigned a) { return __builtin_amdgcn_ds_read_tr16_b64_v4i16((LAS bf16x4*)a); }
__device__ __forceinline__ bf16x8 cat8(bf16x4 a, bf16x4 b) { return __builtin_shufflevector(a, b, 0, 1, 2, 3, 4, 5, 6, 7); }
__device__ __forceinline__ f32x4 mfma16(bf16x8 a, bf16x8 b, f32x4 c) { return __builtin_amdgcn_mfma_f32_16x16x32_bf16(a, b, c, 0, 0, 0); }
typedef float f32x16 __attribute__((ext_vector_type(16)));
typedef __bf16 bfv8 __attribute__((ext_vector_type(8)));
__device__ __forceinline__ f32x16 mfma32(bf16x8 a, bf16x8 b, f32x16 c) {
    return __builtin_amdgcn_mfma_f32_32x32x16_bf16(__builtin_bit_cast(bfv8, a), __builtin_bit_cast(bfv8, b), c, 0, 0, 0);
}
__device__ __forceinline__ unsigned pk_bf16(float lo, float hi) {
    unsigned r; asm("v_cvt_pk_bf16_f32 %0, %1, %2" : "=v"(r) : "v"(lo), "v"(hi)); return r;
}
__device__ __forceinline__ float sigmoidf_(float v) { return __builtin_amdgcn_rcpf(1.0f + __builtin_amdgcn_exp2f(-1.44269504088896f * v)); }
__device__ __forceinline__ float bf_lo(unsigned v) { return __uint_as_float(v << 16); }
__device__ __forceinline__ float bf_hi(unsigned v) { return __uint_as_float(v & 0xffff0000u); }

__device__ __forceinline__ int swz_k256(int k) { return (((k & 3) | (((k >> 3) & 1) << 2)) << 1); }
__device__ __forceinline__ int swz_k128(int k) { return ((((k >> 1) & 1) | (((k >> 3) & 1) << 1)) << 1); }


struct Params {
    const bf16_t* x; const bf16_t* cr; const bf16_t* ci; const bf16_t* hr; const bf16_t* hi;
    const bf16_t* wg; const bf16_t* wu; const bf16_t* wd; const bf16_t* gw;
    bf16_t* ab; float* g; unsigned* cnt; char* part; bf16_t* out;
};
#define IC(n) std::integral_constant<int, n>{}
template <int N, class F, int... I>
__device__ __forceinline__ void sfor_impl(F&& f, std::integer_sequence<int, I...>) { (f(std::integral_constant<int, I>{}), ...); }
template <int N, class F>
__device__ __forceinline__ void sfor(F&& f) { sfor_impl<N>(f, std::make_integer_sequence<int, N>{}); }

__device__ __forceinline__ u32x4 swap16(u32x2 a, u32x2 b) {
    auto s0 = __builtin_amdgcn_permlane16_swap(a.x, b.x, false, false);
    auto s1 = __builtin_amdgcn_permlane16_swap(a.y, b.y, false, false);
    return (u32x4){s0[0], s1[0], s0[1], s1[1]};
}

#define K2LH 5
constexpr int KB_ = 120;
constexpr int K1_LDS = 163840, K1_MISC = 147456;

constexpr int U_STAGE = 49152;
#define U_SEQ 0xFEDBA9C765832140ull
__device__ __forceinline__ void k1_u(const Params& p, unsigned sbase, int wid, int lane) {
    const int xcd = blockIdx.x & 7, idx = blockIdx.x >> 3;
    const int kh = xcd & 1, rtl = idx >> 2, rt = (xcd >> 1) * 4 + rtl, ct = idx & 3;
    if (wid >= 4) {
        const int lw = wid - 4;
        unsigned va[8], vb[4];
#pragma unroll
        for (int i = 0; i < 8; ++i) {
            const int r = ((lw & 1) * 8 + i) * 8 + (lane >> 3);
            va[i] = r * (S_ * 2) + (((lane & 7) ^ ((r >> 1) & 7)) * 16);
        }
#pragma unroll
        for (int i = 0; i < 4; ++i) {
            const int k = (lw * 4 + i) * 4 + (lane >> 4);
            vb[i] = k * 1024 + (((lane & 15) ^ swz_k256(k)) * 16);
        }
        const char* ba0 = (const char*)((lw < 2 ? p.cr : p.ci) + (size_t)rt * 128 * S_ + kh * 1024);
        const char* bb0 = (const char*)(p.x + (size_t)kh * 1024 * D_ + ct * 128);
        auto issue = [&](int t, int slot) {
            const int e = (int)((U_SEQ >> (4 * t)) & 15);
#define KOFF 0
            const int kt = (((e >> 2) + rtl) & 3) + ((e + ct + KOFF) & 3) * 4;
            const unsigned sb = sbase + slot * U_STAGE;
            const char* ba = ba0 + kt * 128;
            const char* bb = bb0 + (size_t)kt * 65536;
#pragma unroll
            for (int i = 0; i < 4; ++i) dma16(bb, vb[i], sb + 32768 + (lw * 4 + i) * 1024);
#pragma unroll
            for (int i = 0; i < 8; ++i) dma16nt(ba, va[i], sb + (lw * 8 + i) * 1024);
        };
        issue(0, 0); issue(1, 1); issue(2, 2);
        WAIT_VM(24); BAR();
        int slot = 0;
#pragma unroll 1
        for (int t = 0; t < 13; ++t) {
            WAIT_VM(12); BAR();
            issue(t + 3, slot);
            slot = slot == 2 ? 0 : slot + 1;
        }
#define U_PH 0
        constexpr int NPF = 1 + U_PH;
        {
            const char* pc; int rel;
            asm volatile("s_getpc_b64 %0\n.Lipg%=:\n\ts_mov_b32 %1, _Z9k2_kernel6Params-.Lipg%=" : "=s"(pc), "=s"(rel));
            int off = ((lane & 1) * 64 + lw * 16 + idx) * 128;
            if (off + 4 > IPF2) off = 0;
            if (lane < 2) pf_line<0>(pc + rel + off, sbase + K1_MISC + 512);
            const int sl = xcd & 3, n = (idx * 4 + lw) * 64 + lane;
            if (U_PH >= 1) pf_line<0>((const char*)((sl & 1) ? p.hi : p.hr) + (size_t)n * 128, sbase + K1_MISC + 512);
            if (U_PH >= 2) {
                const int m = n - K2LH * 512;
                const char* a = m < 0 ? (const char*)p.wd + (size_t)(sl * K2LH * 512 + n) * 128
                                      : (const char*)p.wd + (size_t)(4 * K2LH * 64 + (m >> 2)) * 1024 + (m & 3) * 256 + (sl & 1) * 128;
                if (m >= (28 - 4 * K2LH) * 256) a = (const char*)p.wd;
                pf_line<0>(a, sbase + K1_MISC + 512);
            }
        }
        WAIT_VM(12 + NPF); BAR();
        WAIT_VM(NPF); BAR();
        WAIT_VM(0);
        return;
    }
    const int wm = wid >> 1, wn = wid & 1;
    const int fr = lane & 15, q = lane >> 4, qq = fr >> 2, pp = lane & 3;
    unsigned aM[2], aN[4];
#pragma unroll
    for (int kk = 0; kk < 2; ++kk) aM[kk] = sbase + wm * 16384 + fr * 128 + (((kk * 4 + q) ^ (fr >> 1)) * 16);
#pragma unroll
    for (int ni = 0; ni < 4; ++ni) aN[ni] = sbase + 32768 + (8 * q + qq) * 256 + 32 * ((wn * 4 + ni) ^ (qq | ((q & 1) << 2))) + 8 * pp;
    f32x4 acc[4][8];
#pragma unroll
    for (int a = 0; a < 4; ++a)
#pragma unroll
        for (int b = 0; b < 8; ++b) acc[a][b] = (f32x4){0.f, 0.f, 0.f, 0.f};
    bf16x8 M[8];
    bf16x4 N0[4][2], N1[4][2];
    auto rdM = [&](int mi, int kk) { M[mi] = lds_row_frag(aM[kk] + mi * 2048); };
    auto rdN = [&](bf16x4(&N)[4][2], int ni, int h, int kk) { N[ni][h] = lds_tr(aN[ni] + kk * 8192 + h * 1024); };
    auto grp = [&](bf16x4(&N)[4][2], int mi, auto&& fill) {
#pragma unroll
        for (int ni = 0; ni < 4; ++ni) { SB(); acc[ni][mi] = mfma16(cat8(N[ni][0], N[ni][1]), M[mi], acc[ni][mi]); SB(); fill(ni); }
    };
    BAR();
#pragma unroll
    for (int ni = 0; ni < 4; ++ni) { rdN(N0, ni, 0, 0); rdN(N0, ni, 1, 0); }
#pragma unroll
    for (int mi = 0; mi < 8; ++mi) rdM(mi, 0);
    auto step = [&](int delta, auto lastc) {
        constexpr bool last = decltype(lastc)::value;
#pragma unroll
        for (int mi = 0; mi < 8; ++mi)
            grp(N0, mi, [&](int ni) {
                if (ni == 0 && mi > 0) rdM(mi - 1, 1);
                if (mi < 4 && (ni == 1 || ni == 2)) rdN(N1, mi, ni - 1, 1);
                if (!last && mi >= 4 && ni == 2) ADV(aN[mi - 4], delta);
            });
#pragma unroll
        for (int mi = 0; mi < 8; ++mi) {
            grp(N1, mi, [&](int ni) {
                if (mi == 0 && ni == 0) rdM(7, 1);
                if (!last) {
                    if (mi == 0 && ni >= 2) ADV(aM[ni - 2], delta);
                    if (mi == 2) { if (ni < 2) rdM(ni, 0); else rdN(N0, 0, ni - 2, 0); }
                    if (mi > 2 && ni == 0) rdM(mi - 1, 0);
                    if (mi > 2 && mi < 6 && (ni == 1 || ni == 2)) rdN(N0, mi - 2, ni - 1, 0);
                    if (mi == 7 && ni == 3) rdM(7, 0);
                }
            });
            if (mi == 1 && !last) { SB(); WAIT_LGKM0(); BAR(); }
        }
    };
    int sl3 = 0;
#pragma unroll 1
    for (int t = 0; t < 15; ++t) {
        step(sl3 == 2 ? -2 * U_STAGE : U_STAGE, IC(0));
        sl3 = sl3 == 2 ? 0 : sl3 + 1;
    }
    step(0, IC(1));
    SB();
    {
        const float sg = wm == 0 ? 1.f : -1.f;
        char* dst = (char*)p.ab + ((size_t)(rt * 8 * KB_ + (kh * 2 + wm) * 16 + ct * 4 + wn * 2) * 1024 + (fr + 16 * ((q >> 1) | ((q & 1) << 1))) * 16);
#pragma unroll
        for (int mi = 0; mi < 8; ++mi)
#pragma unroll
            for (int j = 0; j < 2; ++j) {
                const f32x4 v0 = acc[2 * j][mi] * sg, v1 = acc[2 * j + 1][mi] * sg;
                u32x2 w0, w1;
                w0.x = pk_bf16(v0[0], v0[1]); w0.y = pk_bf16(v0[2], v0[3]);
                w1.x = pk_bf16(v1[0], v1[1]); w1.y = pk_bf16(v1[2], v1[3]);
                st16(dst + (size_t)(mi * KB_ + j) * 1024, swap16(w0, w1));
            }
    }
}

constexpr int F_STAGE = 32768;
constexpr int FB[9] = {98304, 114688, 131072, 65536, 81920, 0, 16384, 32768, 49152};
template <int CG>
__device__ __forceinline__ void k1_f(const Params& p, unsigned sbase, int rg, int lane) {
    const int wid = CG * 4 + rg;
    const int xcd = blockIdx.x & 7, item = (xcd >> 1) * 14 + (blockIdx.x >> 3) - 16;
    const int rtl = item & 7, u0 = (item >> 3) * 4, rt = (xcd & 1) * 8 + rtl;
    const int rot = rtl;
    const int l8 = lane >> 3, l7 = lane & 7;
    unsigned vx[2], vb[2];
#pragma unroll
    for (int i = 0; i < 2; ++i) {
        const int r = (wid * 2 + i) * 8 + l8, k = (rg * 2 + i) * 8 + l8;
        vx[i] = r * (D_ * 2) + ((l7 ^ ((r >> 1) & 7)) * 16);
        vb[i] = k * (F_ * 2) + ((l7 ^ ((k & 2) << 1)) * 16);
    }
    const char* bx = (const char*)(p.x + (size_t)rt * 128 * D_);
    const char* bb = (const char*)((CG == 0 ? p.wg : p.wu) + u0 * 64);
    auto issueA = [&](int t, int piece) {
        const unsigned sb = sbase + (t % 3) * F_STAGE;
        const int kt = (t + rot) & 7;
        if (piece < 2) dma16(bb + (size_t)(kt * (64 * F_ * 2)), vb[piece], sb + 16384 + CG * 8192 + (rg * 2 + piece) * 1024);
        else dma16(bx + kt * 128, vx[piece - 2], sb + (wid * 2 + piece - 2) * 1024);
    };
    auto issueB = [&](int s, int piece) {
        const int kt = (s + rot) & 7;
        dma16(bb + (size_t)(kt * (64 * F_ * 2) + (1 + (s >> 3)) * 128), vb[piece], sbase + FB[s % 9] + CG * 8192 + (rg * 2 + piece) * 1024);
    };
#pragma unroll
    for (int t = 0; t < 3; ++t)
#pragma unroll
        for (int i = 0; i < 4; ++i) issueA(t, i);
    const int r32 = lane & 31, hi = lane >> 5, qq = (lane & 15) >> 2, pp = lane & 3;
    unsigned aM[4], aG;
#pragma unroll
    for (int k = 0; k < 4; ++k) aM[k] = sbase + (rg * 32 + r32) * 128 + (((2 * k + hi) ^ ((r32 >> 1) & 7)) * 16);
    aG = sbase + 16384 + (8 * hi + qq) * 128 + 32 * ((2 * CG + ((lane >> 4) & 1)) ^ (qq & 2)) + 8 * pp;
    f32x16 acc[2][2];
    bf16x8 xf[8][4];
    bf16x4 N[4][2];
    auto rdX = [&](int t, int k) { xf[t][k] = lds_row_frag(aM[k]); };
    auto rdW = [&](int i) {
        N[i & 3][0] = lds_tr(aG + (i & 1) * 8192 + (i >> 1) * 2048);
        N[i & 3][1] = lds_tr(aG + (i & 1) * 8192 + (i >> 1) * 2048 + 512);
    };
    auto mm = [&](int par, int t, int i) {
        const f32x16 z = {0.f, 0.f, 0.f, 0.f, 0.f, 0.f, 0.f, 0.f, 0.f, 0.f, 0.f, 0.f, 0.f, 0.f, 0.f, 0.f};
        acc[par][i & 1] = mfma32(cat8(N[i & 3][0], N[i & 3][1]), xf[t][i >> 1], (t == 0 && i < 2) ? z : acc[par][i & 1]);
    };
    auto pfc = [&](int a) {
        const int n = (((blockIdx.x >> 3) - 16) * 8 + wid) * 37 + lane, tl = n >> 8, rl = tl >> 2;
        const int row = ((xcd >> 1) * 4 + rl) * 128 + (n & 127), kt = ((a + rl) & 3) * 4 + (tl & 3);
        const char* g = (const char*)(((n & 128) ? p.ci : p.cr) + (size_t)row * S_ + (xcd & 1) * 1024 + kt * 64);
        if (lane < 37 && n < 4096) pf_line<0>(g, sbase + K1_MISC + 512);
    };
    WAIT_VM(8); BAR();
    rdX(0, 0); rdW(0); rdW(1); rdX(0, 1); rdW(2); rdW(3);
    sfor<8>([&](auto tc) {
        constexpr int t = decltype(tc)::value;
        constexpr int delta = t % 3 == 2 ? -2 * F_STAGE : F_STAGE;
        auto dma = [&](int u, int j) {
            if (u < 0) return;
            if (u <= 4) issueA(u + 3, j);
            if (u == 4 && j == 3) { issueB(0, 0); issueB(0, 1); }
            if ((u == 5 || u == 6) && j < 3) { issueB(1 + (u - 5) * 3 + j, 0); issueB(1 + (u - 5) * 3 + j, 1); }
        };
#pragma unroll
        for (int i = 0; i < 6; ++i) {
            SB(); mm(0, t, i); SB();
            if (i == 0 || i == 2) rdX(t, 2 + (i >> 1));
            if (i < 4) rdW(i + 4);
            if (i < 2) dma(t - 1, 2 + i);
            if (i == 4) { const int d = t < 7 ? delta : FB[0] - F_STAGE - 16384; ADV(aG, d); if (t < 7) { ADV(aM[0], delta); ADV(aM[1], delta); } }
            if (i == 5 && t < 7) { ADV(aM[2], delta); ADV(aM[3], delta); }
        }
        SB();
        constexpr int W[8] = {4, 4, 4, 4, 4, 6, 8, 8};
        WAIT_LGKM0(); WAIT_VM(W[t]); BAR();
        if (t < 7) rdX(t + 1, 0);
        rdW(0); rdW(1);
#pragma unroll
        for (int i = 6; i < 8; ++i) {
            SB(); mm(0, t, i); SB();
            if (i == 6 && t < 7) rdX(t + 1, 1);
            rdW(i - 4);
            if (t < 7) dma(t, i - 6); else { issueB(1 + i, 0); issueB(1 + i, 1); }
        }
    });
    char* hb = (char*)p.ab + ((size_t)((rt * 8 + rg * 2 + (r32 >> 4)) * KB_ + 64 + 2 * u0 + CG) * 1024 + (hi * 16 + (lane & 15)) * 16);
    auto pfk2 = [&]() {
        const int n = (((blockIdx.x >> 3) - 16) * 8 + wid) * 64 + lane, sl = xcd & 3;
        const char* a;
        if (n < 4096) a = (const char*)((sl & 1) ? p.hi : p.hr) + n * 128;
        else if (n < 4096 + K2LH * 512) a = (const char*)p.wd + (size_t)(sl * K2LH * 512 + n - 4096) * 128;
        else a = (const char*)p.wd + (size_t)(4 * K2LH * 512 + (n - 4096 - K2LH * 512) * 2 + (sl & 1)) * 128;
        pf_line<0>(a, sbase + K1_MISC + 512);
    };
    u32x2 w[4];
    float e[4];
#define PIN(v) asm volatile("" : "+v"(v))
    auto epi = [&](int par, int n) {
        if (n >= 88) return;
        const int c = n / 22, o = n % 22, i = o & 3;
        if (o < 4) e[i] = -1.44269504088896f * acc[par][0][4 * c + i];
        else if (o < 8) e[i] = __builtin_amdgcn_exp2f(e[i]);
        else if (o < 12) e[i] = 1.0f + e[i];
        else if (o < 16) e[i] = __builtin_amdgcn_rcpf(e[i]);
        else if (o < 20) e[i] = e[i] * acc[par][1][4 * c + i];
        else if (o == 20) { w[c].x = pk_bf16(e[0], e[1]); PIN(w[c].x); }
        else { w[c].y = pk_bf16(e[2], e[3]); PIN(w[c].y); }
        if (o < 20) PIN(e[i]);
    };
    auto st_h = [&](int u, int gp) {
        auto s0 = __builtin_amdgcn_permlane32_swap(w[2 * gp].x, w[2 * gp + 1].x, false, false);
        auto s1 = __builtin_amdgcn_permlane32_swap(w[2 * gp].y, w[2 * gp + 1].y, false, false);
        st16(hb + 2 * u * 1024 + gp * 512, (u32x4){s0[0], s1[0], s0[1], s1[1]});
    };
    sfor<24>([&](auto sc) {
        constexpr int s = decltype(sc)::value, t = s & 7, par = (1 + (s >> 3)) & 1;
        constexpr int delta = s < 23 ? FB[(s + 1) % 9] - FB[s % 9] : 0;
        constexpr bool bar = s % 3 == 2 && s < 23;
#pragma unroll
        for (int i = 0; i < 8; ++i) {
            SB(); mm(par, t, i); SB();
            if (i < 4) rdW(i + 4);
            if (i == 3 && s < 23) ADV(aG, delta);
            if (bar && i == 5) {
                constexpr int WB[8] = {6, 6, 8, 6, 8, 8, 0, 0};
                WAIT_LGKM0(); WAIT_VM(WB[s / 3]); BAR();
                rdW(0);
            }
            if (i >= 4 && s < 23 && !(bar && i == 4)) rdW(i - 4);
            epi(par ^ 1, t * 16 + 2 * i); epi(par ^ 1, t * 16 + 2 * i + 1);
            if (t == 6 && (i == 1 || i == 3)) st_h(s >> 3, i >> 1);
            if constexpr (s % 3 == 2 && s + 7 < 24) { if (i >= 6) { issueB(s + 7, i & 1); } }
            if constexpr (s % 3 == 0 && s >= 3 && s + 6 < 24) { if (i < 4) issueB(s + 7 + (i >> 1), i & 1); }
        }
    });
    SB();
#pragma unroll
    for (int n = 0; n < 88; ++n) {
        epi(1, n);
        if (n == 43) st_h(3, 0);
    }
    st_h(3, 1);
    WAIT_VM(0);
}

__device__ __forceinline__ void k1_s(const Params& p, unsigned sbase, int wid, int lane) {
    const int xcd = blockIdx.x & 7, j = (blockIdx.x >> 3) - 30, sidx = xcd * 2 + j;
    const int row0 = (xcd & 1) * 1024 + (xcd >> 1) * 256 + j * 128 + wid * 16;
    {
        int n = (j * 8 + wid) * 64 + lane;
        const char* a = (const char*)p.cr;
        if (n >= 128) { a = (const char*)p.ci; n -= 128;
        if (n >= 128) { a = (const char*)p.ab; n -= 128;
        if (n >= 448) { a = (const char*)p.out; n -= 448;
        if (n >= 32) { a = (const char*)p.wd; n -= 32;
        if (n >= 28) { a = (const char*)p.wg; n -= 28;
        if (n >= 28) { a = (const char*)p.wu; n -= 28;
        if (n >= 28) { a = (const char*)p.hr; n -= 28;
        if (n >= 8) { a = (const char*)p.hi; n -= 8;
        if (n >= 8) { a = (const char*)p.x; n = (n - 8) & 31; } } } } } } } } }
        pf_line<TLBT>(a + (size_t)n * 65536, sbase);
    }
    {
        const char* pc; int rel, len;
        asm volatile("s_getpc_b64 %0\n.Liph%=:\n\ts_mov_b32 %1, _Z9k1_kernel6Params-.Liph%=\n\ts_mov_b32 %2, _Z9k2_kernel6Params-_Z9k1_kernel6Params"
                     : "=s"(pc), "=s"(rel), "=s"(len));
        const int off = ((lane * 8 + wid) * 2 + j) * 128;
        if (off + 4 <= len + IPFS) pf_line<0>(pc + rel + off, sbase);
    }
    auto pfb = [&]() {
        const int sl = xcd & 3, n0 = (j * 8 + wid) * 64 + lane;
        const char* hm = (const char*)((sl & 1) ? p.hi : p.hr);
        constexpr int N2 = (28 - 4 * K2LH) * 256, N1 = K2LH * 512;
#pragma unroll
        for (int i = 0; i < N2 / 1024; ++i) {
            const int n = (i * 1024 + n0 + (xcd >> 1) * (N2 / 4)) % N2;
            pf_line<0>((const char*)p.wd + (size_t)(4 * K2LH * 64 + (n >> 2)) * 1024 + (n & 3) * 256 + (sl & 1) * 128, sbase);
        }
#pragma unroll
        for (int i = 0; i < (K2LH + 1) / 2; ++i) {
            const int n = (i * 1024 + n0 + (xcd >> 2) * (N1 / 2)) % N1;
            if (i * 1024 + n0 < N1) pf_line<0>((const char*)p.wd + (size_t)(sl * N1 + n) * 128, sbase);
        }
#pragma unroll
        for (int i = 0; i < 4; ++i) pf_line<0>(hm + (size_t)(((i + (xcd >> 1)) & 3) * 1024 + n0) * 128, sbase);
    };
    pfb();
    const u32x4 gw = *(const u32x4*)(p.gw + lane * 8);
    u32x4 xv[16];
#pragma unroll
    for (int i = 0; i < 16; ++i) xv[i] = *(const u32x4*)(p.x + (size_t)(row0 + i) * D_ + lane * 8);
    float mine = 0.f;
#pragma unroll
    for (int i = 0; i < 16; ++i) {
        float a = 0.f;
#pragma unroll
        for (int k = 0; k < 4; ++k) a += bf_lo(xv[i][k]) * bf_lo(gw[k]) + bf_hi(xv[i][k]) * bf_hi(gw[k]);
#pragma unroll
        for (int o = 32; o > 0; o >>= 1) a += __shfl_xor(a, o);
        if (lane == i) mine = a;
    }
    if (lane < 16) p.g[row0 + lane] = sigmoidf_(mine);
    else if (lane < 18) p.cnt[(sidx * 16 + wid * 2 + lane - 16) * 32] = 0u;
    WAIT_VM(0);
}

__global__ __launch_bounds__(512, 2) void k1_kernel(Params p) {
    extern __shared__ __attribute__((aligned(16))) char smem_g[];
    const unsigned sbase = (unsigned)(size_t)(lds_ptr)smem_g;
    const int lane = threadIdx.x & 63;
    const int wid = __builtin_amdgcn_readfirstlane(threadIdx.x >> 6);
    const int idx = blockIdx.x >> 3;
    if (idx < 16) k1_u(p, sbase, wid, lane);
    else if (idx < 30) { if (wid < 4) k1_f<0>(p, sbase, wid, lane); else k1_f<1>(p, sbase, wid - 4, lane); }
    else k1_s(p, sbase, wid, lane);
}

constexpr int K2_LH = K2LH, K2_L = 8 + K2_LH, K2_M = 28 - 4 * K2_LH;
constexpr int K2_ST = 4, K2_STAGE = 32768, K2_P2 = K2_M / 2, K2_NT = K2_L + K2_P2;
constexpr int K2_OWN = K2_ST * K2_STAGE;

__global__ __launch_bounds__(512, 2) void k2_kernel(Params p) {
    extern __shared__ __attribute__((aligned(16))) char smem_g[];
    const unsigned sbase = (unsigned)(size_t)(lds_ptr)smem_g;
    const int lane = threadIdx.x & 63;
    const int wid = __builtin_amdgcn_readfirstlane(threadIdx.x >> 6);
    const int xcd = blockIdx.x & 7, idx = blockIdx.x >> 3;
    const int sl = xcd & 3, rt = (xcd >> 2) * 8 + (idx >> 2), ct = idx & 3, tile = rt * 4 + ct;
    const int row0 = rt * 128, col0 = ct * 128;
    const int row2 = row0 + (sl >> 1) * 64, col2 = col0 + (sl & 1) * 64;
    if (wid >= 4) {
        const int lw = wid - 4;
        unsigned va[4], vbH[4], vbW[4], va2[4], vb2[4];
#pragma unroll
        for (int i = 0; i < 4; ++i) {
            const int piece = lw * 4 + i;
            const int r = piece * 8 + (lane >> 3), c = (lane & 7) ^ ((r >> 1) & 7);
            va[i] = ((piece >> 1) * KB_ + (piece & 1)) * 1024 + lane * 16;
            vbH[i] = r * (D_ * 2) + c * 16;
            const int k = piece * 4 + (lane >> 4), cc = (lane & 15) ^ swz_k256(k);
            vbW[i] = k * 1024 + cc * 16;
            const int sub = piece >> 3, r2 = (piece & 7) * 8 + (lane >> 3);
            va2[i] = (lw * KB_ + i) * 1024 + lane * 16;
            vb2[i] = (sub * 64 + r2) * 1024 + (((lane & 7) ^ swz_k128(r2)) * 16);
        }
        const int rot1 = (idx >> 2) + 2 * ct, rot2 = ((idx >> 2) + ct) % K2_LH, rot3 = ((idx >> 2) + ct) % K2_P2;
        auto issue = [&](int t, int slot) {
            const unsigned sb = sbase + slot * K2_STAGE + lw * 4096;
            if (t < K2_L) {
                const char* ba; const char* bb;
                if (t < 8) {
                    const int j = (t + rot1) & 7;
                    ba = (const char*)p.ab + (size_t)(rt * 8 * KB_ + sl * 16 + 2 * j) * 1024;
                    bb = (const char*)(((sl & 1) ? p.hi : p.hr) + (size_t)col0 * D_ + j * 64);
#pragma unroll
                    for (int i = 0; i < 4; ++i) dma16(bb, vbH[i], sb + 16384 + i * 1024);
                } else {
                    const int kf = sl * K2_LH + (t - 8 + rot2) % K2_LH;
                    ba = (const char*)p.ab + (size_t)(rt * 8 * KB_ + 64 + 2 * kf) * 1024;
                    bb = (const char*)(p.wd + (size_t)kf * 64 * D_ + col0);
#pragma unroll
                    for (int i = 0; i < 4; ++i) dma16(bb, vbW[i], sb + 16384 + i * 1024);
                }
#pragma unroll
                for (int i = 0; i < 4; ++i) dma16(ba, va[i], sb + i * 1024);
            } else {
                const int kf = 4 * K2_LH + ((t - K2_L + rot3) % K2_P2) * 2;
                const char* ba = (const char*)p.ab + (size_t)((row2 >> 4) * KB_ + 64 + 2 * kf) * 1024;
                const char* bb = (const char*)(p.wd + (size_t)kf * 64 * D_ + col2);
#pragma unroll
                for (int i = 0; i < 4; ++i) dma16(bb, vb2[i], sb + 16384 + i * 1024);
#pragma unroll
                for (int i = 0; i < 4; ++i) dma16(ba, va2[i], sb + i * 1024);
            }
        };
        issue(0, 0); issue(1, 1);
        WAIT_VM(8); BAR();
        issue(2, 2); issue(3, 3);
#pragma unroll 1
        for (int t = 0; t < K2_NT - K2_ST; ++t) {
            WAIT_VM(16); BAR();
            issue(t + K2_ST, t & 3);
        }
        WAIT_VM(16); BAR();
        WAIT_VM(8); BAR();
        WAIT_VM(0); BAR();
        if (lw == 0) {
            const unsigned* c = p.cnt + (tile * 4 + sl) * 32;
            for (int spin = 0; spin < (1 << 20); ++spin) {
                const unsigned v = __hip_atomic_load(c, __ATOMIC_RELAXED, __HIP_MEMORY_SCOPE_AGENT);
                if (v >= 3u) break;
                __builtin_amdgcn_s_sleep(2);
            }
        }
        BAR();
        return;
    }
    const int wm = wid >> 1, wn = wid & 1;
    const int fr = lane & 15, q = lane >> 4, qq = fr >> 2, pp = lane & 3;
    float g1[4], g2[2];
#pragma unroll
    for (int mi = 0; mi < 4; ++mi) g1[mi] = p.g[row0 + wm * 64 + mi * 16 + fr];
#pragma unroll
    for (int mi = 0; mi < 2; ++mi) g2[mi] = p.g[row2 + wm * 32 + mi * 16 + fr];
    {
        unsigned aM = sbase + wm * 8192 + lane * 16, aH[2], aN[4];
#pragma unroll
        for (int kk = 0; kk < 2; ++kk) aH[kk] = sbase + 16384 + (wn * 64 + fr) * 128 + (((kk * 4 + q) ^ (fr >> 1)) * 16);
#pragma unroll
        for (int ni = 0; ni < 4; ++ni) aN[ni] = sbase + 16384 + (8 * q + qq) * 256 + 32 * ((wn * 4 + ni) ^ (qq | ((q & 1) << 2))) + 8 * pp;
        f32x4 acc[4][4], accS[4][4];
#pragma unroll
        for (int a = 0; a < 4; ++a)
#pragma unroll
            for (int b = 0; b < 4; ++b) acc[a][b] = (f32x4){0.f, 0.f, 0.f, 0.f};
        bf16x8 M0[4], M1[4];
        bf16x4 N0[4][2], N1[4][2];
        auto rd = [&](int ty, bf16x8(&M)[4], bf16x4(&N)[4][2], int j, int kk) {
            const int nr = ty == 0 ? 8 : 12, m0 = ty == 0 ? 1 : 2;
            if (j >= nr) return;
            if (j == m0) M[0] = lds_row_frag(aM + kk * 1024);
            else if (j >= nr - 3) M[j - nr + 4] = lds_row_frag(aM + (j - nr + 4) * 2048 + kk * 1024);
            else {
                const int n = j > m0 ? j - 1 : j;
                if (ty == 0) {
                    const bf16x8 v = lds_row_frag(aH[kk] + n * 2048);
                    N[n][0] = __builtin_shufflevector(v, v, 0, 1, 2, 3); N[n][1] = __builtin_shufflevector(v, v, 4, 5, 6, 7);
                } else N[n >> 1][n & 1] = lds_tr(aN[n >> 1] + kk * 8192 + (n & 1) * 1024);
            }
        };
        auto mm = [&](bf16x8(&M)[4], bf16x4(&N)[4][2], int i) { acc[i & 3][i >> 2] = mfma16(cat8(N[i & 3][0], N[i & 3][1]), M[i >> 2], acc[i & 3][i >> 2]); };
        auto step = [&](int delta, auto tyc, auto tnc) {
            constexpr int ty = decltype(tyc)::value, tn = decltype(tnc)::value;
#pragma unroll
            for (int i = 0; i < 16; ++i) {
                SB(); mm(M0, N0, i); SB();
                rd(ty, M1, N1, i, 1);
                if (i >= 12) { if (ty == 1) ADV(aN[i - 12], delta); else if (i < 14) ADV(aH[i - 12], delta); }
            }
            SB(); mm(M1, N1, 0); SB(); ADV(aM, delta); SB(); mm(M1, N1, 1); SB();
            WAIT_LGKM0(); BAR();
#pragma unroll
            for (int i = 2; i < 16; ++i) {
                SB(); mm(M1, N1, i); SB();
                if (tn < 2) rd(tn, M0, N0, i - 2, 0);
            }
        };
        BAR();
#pragma unroll
        for (int j = 0; j < 8; ++j) rd(0, M0, N0, j, 0);
#pragma unroll 1
        for (int t = 0; t < 7; ++t) step((t & 3) == 3 ? -3 * K2_STAGE : K2_STAGE, IC(0), IC(0));
        step(-3 * K2_STAGE, IC(0), IC(1));
#pragma unroll
        for (int b = 0; b < 4; ++b)
#pragma unroll
            for (int a = 0; a < 4; ++a) { accS[a][b] = acc[a][b] * g1[b]; acc[a][b] = (f32x4){0.f, 0.f, 0.f, 0.f}; }
#pragma unroll 1
        for (int t = 8; t < K2_L - 1; ++t) step((t & 3) == 3 ? -3 * K2_STAGE : K2_STAGE, IC(1), IC(1));
        step(0, IC(1), IC(2));
        SB();
#pragma unroll
        for (int b = 0; b < 4; ++b) {
            const float f = 1.f - g1[b];
#pragma unroll
            for (int a = 0; a < 4; ++a) accS[a][b] += acc[a][b] * f;
        }
        if (wid == sl) {
#pragma unroll
            for (int mi = 0; mi < 4; ++mi)
#pragma unroll
                for (int ni = 0; ni < 4; ++ni)
                    *(LAS f32x4*)(sbase + K2_OWN + (((((mi >> 1) * 2 + (ni >> 1)) * 4 + (mi & 1) * 2 + (ni & 1)) * 64 + lane) * 16)) = accS[ni][mi];
        } else {
            char* pb = p.part + ((size_t)((tile * 4 + wid) * 4 + sl)) * 8192 + lane * 16;
#pragma unroll
            for (int mi = 0; mi < 4; ++mi)
#pragma unroll
                for (int nh = 0; nh < 2; ++nh) {
                    const f32x4 v0 = accS[2 * nh][mi], v1 = accS[2 * nh + 1][mi];
                    u32x4 w;
                    w.x = pk_bf16(v0[0], v0[1]); w.y = pk_bf16(v0[2], v0[3]); w.z = pk_bf16(v1[0], v1[1]); w.w = pk_bf16(v1[2], v1[3]);
                    asm volatile("global_store_dwordx4 %0, %1, off sc1\n\ts_nop 1" ::"v"(pb + ((((mi >> 1) * 2 + nh) * 2 + (mi & 1)) * 1024)), "v"(w) : "memory");
                }
        }
    }
    unsigned bW[2];
    const unsigned bM = sbase + wm * 8192 + lane * 16;
    {
        const int f = (qq >> 1) | ((q & 1) << 1);
#pragma unroll
        for (int ni = 0; ni < 2; ++ni) bW[ni] = sbase + 16384 + (8 * q + qq) * 128 + 32 * ((wn * 2 + ni) ^ f) + 8 * pp;
    }
    f32x4 acc2[2][2];
#pragma unroll
    for (int a = 0; a < 2; ++a)
#pragma unroll
        for (int b = 0; b < 2; ++b) acc2[a][b] = (f32x4){0.f, 0.f, 0.f, 0.f};
    bf16x8 P0[2][2], Q0[2][2], P1[2][2], Q1[2][2];
    auto rd2 = [&](bf16x8(&M)[2][2], bf16x8(&N)[2][2], int j, unsigned off, int sub) {
        const int kk = j >> 2, i = j & 1;
        if (j & 2) M[kk][i] = lds_row_frag(bM + off + i * 4096 + sub * 2048 + kk * 1024);
        else N[kk][i] = cat8(lds_tr(bW[i] + off + sub * 8192 + kk * 4096), lds_tr(bW[i] + off + sub * 8192 + kk * 4096 + 512));
    };
    auto mm2 = [&](bf16x8(&M)[2][2], bf16x8(&N)[2][2], int i) {
        acc2[i & 1][(i >> 1) & 1] = mfma16(N[i >> 2][i & 1], M[i >> 2][(i >> 1) & 1], acc2[i & 1][(i >> 1) & 1]);
    };
#pragma unroll
    for (int j = 0; j < 8; ++j) rd2(P0, Q0, j, (K2_L & 3) * K2_STAGE, 0);
#pragma unroll
    for (int t = 0; t < K2_P2; ++t) {
        const unsigned off = ((K2_L + t) & 3) * K2_STAGE, offn = ((K2_L + t + 1) & 3) * K2_STAGE;
#pragma unroll
        for (int i = 0; i < 8; ++i) { SB(); mm2(P0, Q0, i); SB(); rd2(P1, Q1, i, off, 1); }
        SB(); mm2(P1, Q1, 0); mm2(P1, Q1, 1); SB();
        if (t == (K2_P2 > 1 ? 1 : 0)) {
            WAIT_VM(0);
            if (wid != sl && lane == 0) __hip_atomic_fetch_add(p.cnt + (tile * 4 + wid) * 32, 1u, __ATOMIC_RELAXED, __HIP_MEMORY_SCOPE_AGENT);
        }
        WAIT_LGKM0(); BAR();
        if (t + 1 < K2_P2) {
#pragma unroll
            for (int i = 2; i < 8; ++i) {
                SB(); mm2(P1, Q1, i); SB();
                rd2(P0, Q0, i - 2, offn, 0);
                if (i >= 6) rd2(P0, Q0, i, offn, 0);
            }
        }
    }
    u32x4 pv[3][2];
    {
        const char* pb = p.part + ((size_t)((tile * 4 + sl) * 4)) * 8192 + wid * 2048 + lane * 16;
        const int s0 = sl == 0 ? 1 : 0, s1 = sl <= 1 ? 2 : 1, s2 = sl <= 2 ? 3 : 2;
        const char* p0 = pb + s0 * 8192; const char* p1 = pb + s1 * 8192; const char* p2 = pb + s2 * 8192;
        asm volatile("global_load_dwordx4 %0, %6, off sc1\n\tglobal_load_dwordx4 %1, %6, off offset:1024 sc1\n\t"
                     "global_load_dwordx4 %2, %7, off sc1\n\tglobal_load_dwordx4 %3, %7, off offset:1024 sc1\n\t"
                     "global_load_dwordx4 %4, %8, off sc1\n\tglobal_load_dwordx4 %5, %8, off offset:1024 sc1"
                     : "=&v"(pv[0][0]), "=&v"(pv[0][1]), "=&v"(pv[1][0]), "=&v"(pv[1][1]), "=&v"(pv[2][0]), "=&v"(pv[2][1])
                     : "v"(p0), "v"(p1), "v"(p2) : "memory");
    }
#pragma unroll
    for (int i = 2; i < 8; ++i) { SB(); mm2(P1, Q1, i); SB(); }
    f32x4 own[2][2];
#pragma unroll
    for (int mi = 0; mi < 2; ++mi)
#pragma unroll
        for (int ni = 0; ni < 2; ++ni) own[ni][mi] = *(LAS f32x4*)(sbase + K2_OWN + ((wid * 4 + mi * 2 + ni) * 64 + lane) * 16);
    asm volatile("s_waitcnt vmcnt(0)" : "+v"(pv[0][0]), "+v"(pv[0][1]), "+v"(pv[1][0]), "+v"(pv[1][1]), "+v"(pv[2][0]), "+v"(pv[2][1]) :: "memory");
    const unsigned st = sbase + wid * 2048;
#pragma unroll
    for (int mi = 0; mi < 2; ++mi)
#pragma unroll
        for (int ni = 0; ni < 2; ++ni) {
            f32x4 v = own[ni][mi] + acc2[ni][mi] * (1.f - g2[mi]);
#pragma unroll
            for (int s = 0; s < 3; ++s) {
                v[0] += bf_lo(pv[s][mi][2 * ni]); v[1] += bf_hi(pv[s][mi][2 * ni]);
                v[2] += bf_lo(pv[s][mi][2 * ni + 1]); v[3] += bf_hi(pv[s][mi][2 * ni + 1]);
            }
            u32x2 w; w.x = pk_bf16(v[0], v[1]); w.y = pk_bf16(v[2], v[3]);
            *(LAS u32x2*)(st + (mi * 16 + fr) * 64 + (((ni * 2 + (q >> 1)) ^ ((fr >> 1) & 3)) * 16) + (q & 1) * 8) = w;
        }
    {
        const int r = lane >> 2, c = lane & 3;
#pragma unroll
        for (int i = 0; i < 2; ++i) {
            const u32x4 v = *(LAS u32x4*)(st + (i * 16 + r) * 64 + ((c ^ ((r >> 1) & 3)) * 16));
            asm volatile("global_store_dwordx4 %0, %1, off sc1\n\ts_nop 1" ::"v"(p.out + (size_t)(row2 + wm * 32 + i * 16 + r) * D_ + col2 + wn * 32 + c * 8), "v"(v) : "memory");
        }
    }
}

extern "C" void kernel_launch(void* const* d_in, const int* in_sizes, int n_in, void* d_out, int out_size, void* d_ws,
                              size_t ws_size, hipStream_t stream) {
    Params p;
    p.x = (const bf16_t*)d_in[0]; p.cr = (const bf16_t*)d_in[1]; p.ci = (const bf16_t*)d_in[2];
    p.hr = (const bf16_t*)d_in[3]; p.hi = (const bf16_t*)d_in[4];
    p.wg = (const bf16_t*)d_in[6]; p.wu = (const bf16_t*)d_in[7]; p.wd = (const bf16_t*)d_in[8]; p.gw = (const bf16_t*)d_in[9];
    const size_t wneed = (size_t)32 << 20;
    char* ws = (char*)d_ws + (ws_size > wneed ? ((ws_size - wneed) & ~(((size_t)2 << 20) - 1)) : 0);
    p.ab = (bf16_t*)ws;
    p.g = (float*)(ws + (16u << 20));
    p.cnt = (unsigned*)(ws + (16u << 20) + 65536);
    p.part = ws + (20u << 20);
    p.out = (bf16_t*)d_out;
    (void)hipFuncSetAttribute((const void*)k1_kernel, hipFuncAttributeMaxDynamicSharedMemorySize, K1_LDS);
    (void)hipFuncSetAttribute((const void*)k2_kernel, hipFuncAttributeMaxDynamicSharedMemorySize, K2_OWN + 16384 + 256);
    k1_kernel<<<256, 512, K1_LDS, stream>>>(p);
    k2_kernel<<<256, 512, K2_OWN + 16384 + 256, stream>>>(p);
}
```
